```python
import torch

def _softmax(x, axis=-1):
    return torch.softmax(x, dim=axis)
def _gelu(x):
    return torch.nn.functional.gelu(x, approximate="tanh")
def _silu(x):
    return torch.nn.functional.silu(x)
def _sigmoid(x):
    return torch.sigmoid(x)
def _layernorm(x, axis=-1, eps=1e-5):
    m = x.mean(dim=axis, keepdim=True)
    v = x.var(dim=axis, keepdim=True, unbiased=False)
    return (x - m) / torch.sqrt(v + eps)
def _scatter_add(tgt, idx, src, axis=0):
    return tgt.clone().index_add_(axis, idx, src)
def _segment_sum(src, seg, n):
    out = torch.zeros((n,) + src.shape[1:], dtype=src.dtype)
    return out.index_add_(0, seg, src)
def _pad(x, pw):
    flat = tuple(v for pair in reversed(pw) for v in pair)
    return torch.nn.functional.pad(x, flat)

kernel_name = 'standalone__glu_gated_glu__decode'

def setup_inputs():

    _w = (lambda a: (lambda t: t.to(torch.bfloat16) if t.is_floating_point() else t)(torch.as_tensor(a)))
    B, S, D, F = 4, 1, 1024, 3584
    return {
        "x": _w((lambda *s: torch.randn(*s, dtype=torch.bfloat16) * 0.1)(B, S, D)),
        "w_gate": _w((lambda *s: torch.randn(*s, dtype=torch.bfloat16) * 0.1)(D, F)),
        "w_up": _w((lambda *s: torch.randn(*s, dtype=torch.bfloat16) * 0.05)(D, F)),
        "w_down": _w((lambda *s: torch.randn(*s, dtype=torch.bfloat16) * 0.05)(F, D)),
    }

def reference(x, w_gate, w_up, w_down):
    g = torch.einsum("bsd,df->bsf", x, w_gate)
    u = torch.einsum("bsd,df->bsf", x, w_up)
    h = _sigmoid(g) * u
    return torch.einsum("bsf,fd->bsd", h, w_down)
```

```cpp
#include <hip/hip_runtime.h>
#include <hip/hip_bf16.h>
#include <stdint.h>

namespace {
constexpr int B = 4, D = 1024, F = 3584;
typedef unsigned v4u __attribute__((ext_vector_type(4)));

__device__ __forceinline__ float bflo(unsigned u) { return __builtin_bit_cast(float, u << 16); }
__device__ __forceinline__ float bfhi(unsigned u) { return __builtin_bit_cast(float, u & 0xFFFF0000u); }

__global__ void __launch_bounds__(256) k_gu(const unsigned short* __restrict__ x, const unsigned short* __restrict__ wg,
                                            const unsigned short* __restrict__ wu, float* __restrict__ h) {
    __shared__ float xs[B][D];
    __shared__ float red[2][B][16][129];
    const int tid = threadIdx.x;
    for (int i = tid; i < B * D; i += 256) xs[i / D][i % D] = bflo(x[i]);
    __syncthreads();
    const int half = tid & 1, r = tid >> 1;
    const int c0 = blockIdx.x * 16 + half * 8;
    float ag[B][8], au[B][8];
    for (int b = 0; b < B; ++b) for (int j = 0; j < 8; ++j) { ag[b][j] = 0.f; au[b][j] = 0.f; }
    for (int k = r; k < D; k += 128) {
        v4u g = *(const v4u*)(wg + (size_t)k * F + c0);
        v4u u = *(const v4u*)(wu + (size_t)k * F + c0);
        for (int b = 0; b < B; ++b) {
            float xv = xs[b][k];
            for (int j = 0; j < 4; ++j) {
                ag[b][2 * j] += xv * bflo(g[j]); ag[b][2 * j + 1] += xv * bfhi(g[j]);
                au[b][2 * j] += xv * bflo(u[j]); au[b][2 * j + 1] += xv * bfhi(u[j]);
            }
        }
    }
    for (int b = 0; b < B; ++b) for (int j = 0; j < 8; ++j) {
        red[0][b][half * 8 + j][r] = ag[b][j];
        red[1][b][half * 8 + j][r] = au[b][j];
    }
    __syncthreads();
    if (tid < 64) {
        int b = tid >> 4, c = tid & 15;
        float g = 0.f, u = 0.f;
        for (int i = 0; i < 128; ++i) { g += red[0][b][c][i]; u += red[1][b][c][i]; }
        h[b * F + blockIdx.x * 16 + c] = u / (1.f + __expf(-g));
    }
}

__global__ void __launch_bounds__(256) k_dn(const float* __restrict__ h, const unsigned short* __restrict__ wd,
                                            float* __restrict__ part) {
    __shared__ float red[B][64][33];
    const int tid = threadIdx.x, cg = blockIdx.x & 15, kg = blockIdx.x >> 4;
    const int c8 = tid & 7, r = tid >> 3;
    float acc[B][8];
    for (int b = 0; b < B; ++b) for (int j = 0; j < 8; ++j) acc[b][j] = 0.f;
    for (int k = r; k < 224; k += 32) {
        int f = kg * 224 + k;
        v4u w = *(const v4u*)(wd + (size_t)f * D + cg * 64 + c8 * 8);
        for (int b = 0; b < B; ++b) {
            float hv = h[b * F + f];
            for (int j = 0; j < 4; ++j) { acc[b][2 * j] += hv * bflo(w[j]); acc[b][2 * j + 1] += hv * bfhi(w[j]); }
        }
    }
    for (int b = 0; b < B; ++b) for (int j = 0; j < 8; ++j) red[b][c8 * 8 + j][r] = acc[b][j];
    __syncthreads();
    {
        int b = tid >> 6, c = tid & 63;
        float s = 0.f;
        for (int i = 0; i < 32; ++i) s += red[b][c][i];
        part[(kg * B + b) * D + cg * 64 + c] = s;
    }
}

__global__ void __launch_bounds__(256) k_red(const float* __restrict__ part, __hip_bfloat16* __restrict__ out) {
    int i = blockIdx.x * 256 + threadIdx.x;
    float s = 0.f;
    for (int kg = 0; kg < 16; ++kg) s += part[kg * B * D + i];
    out[i] = __float2bfloat16(s);
}
}

extern "C" void kernel_launch(void* const* d_in, const int* in_sizes, int n_in, void* d_out, int out_size, void* d_ws,
                              size_t ws_size, hipStream_t stream) {
    const unsigned short* x = (const unsigned short*)d_in[0];
    const unsigned short* wg = (const unsigned short*)d_in[1];
    const unsigned short* wu = (const unsigned short*)d_in[2];
    const unsigned short* wd = (const unsigned short*)d_in[3];
    float* h = (float*)d_ws;
    float* part = h + B * F;
    k_gu<<<224, 256, 0, stream>>>(x, wg, wu, h);
    k_dn<<<256, 256, 0, stream>>>(h, wd, part);
    k_red<<<16, 256, 0, stream>>>(part, (__hip_bfloat16*)d_out);
}
```

```cpp
#include <hip/hip_runtime.h>
#include <stdint.h>

namespace glu {
constexpr int B = 4, D = 1024, F = 3584;
constexpr int NG = F / 64;
constexpr int NWG = NG * 4;
constexpr int NT = 512;
constexpr int S1_OFF = 4096;
constexpr int S2_OFF = S1_OFF + NWG * 256 * 16;
constexpr int WS_BYTES = S2_OFF + NWG * 512 * 16;
constexpr int STAMP_OFF = 64 << 20;
constexpr int AUX_SC1 = 16;
constexpr unsigned SPIN_CAP = 1u << 17;

typedef unsigned v4u __attribute__((ext_vector_type(4)));
typedef unsigned v2u __attribute__((ext_vector_type(2)));
typedef float f32x4 __attribute__((ext_vector_type(4)));
typedef float f32x2 __attribute__((ext_vector_type(2)));
typedef short bf16x8 __attribute__((ext_vector_type(8)));
typedef unsigned short u16;

__device__ __forceinline__ f32x4 mfma(v4u a, v4u b, f32x4 c) {
    return __builtin_amdgcn_mfma_f32_16x16x32_bf16(__builtin_bit_cast(bf16x8, a), __builtin_bit_cast(bf16x8, b), c, 0, 0, 0);
}
__device__ __forceinline__ unsigned bf16_rne(float f) {
    unsigned u = __builtin_bit_cast(unsigned, f);
    u += 0x7FFFu + ((u >> 16) & 1u);
    return u >> 16;
}
__device__ __forceinline__ float as_f(unsigned u) { return __builtin_bit_cast(float, u); }
__device__ __forceinline__ unsigned as_u(float f) { return __builtin_bit_cast(unsigned, f); }

#define STAMP(i) do { } while (0)

__global__ void __launch_bounds__(NT, 2)
glu_kernel(const u16* __restrict__ x, const u16* __restrict__ wg, const u16* __restrict__ wu,
           const u16* __restrict__ wd, u16* __restrict__ out, unsigned char* __restrict__ ws) {
    __shared__ f32x4 red[8 * 2 * 64];
    __shared__ __attribute__((aligned(16))) u16 hs[4 * 64];
    __shared__ float fin[8 * 32];
    const int tid = threadIdx.x, lane = tid & 63;
    const int w = __builtin_amdgcn_readfirstlane(tid >> 6);
    const int bid = blockIdx.x, g = bid >> 2, q = bid & 3;
    STAMP(0);
    const __amdgpu_buffer_rsrc_t rs = __builtin_amdgcn_make_buffer_rsrc((void*)ws, 0, WS_BYTES, 0x00020000);

    const int q4 = lane >> 4, n = lane & 15, b2 = n >> 3, jp = n & 7;
    const unsigned epoch = __builtin_amdgcn_raw_buffer_load_b32(rs, 0, 0, AUX_SC1);
    const int kbase = 256 * q + 32 * w + 8 * q4;
    const v4u xa = *(const v4u*)(x + b2 * D + kbase);
    const v4u xb = *(const v4u*)(x + (b2 + 2) * D + kbase);
    const u16* wsel = (n < 8) ? wg : wu;
    const v4u* p1 = (const v4u*)(wsel + (size_t)kbase * F + g * 64 + (n & 7) * 8);
    v4u w1[8];
#pragma unroll
    for (int i = 0; i < 8; ++i) w1[i] = __builtin_nontemporal_load(p1 + i * (F / 8));
    const int ch = w & 1, rg = w >> 1;
    const v4u* p2 = (const v4u*)(wd + (size_t)(64 * g + 16 * rg + 4 * q4) * D + 256 * q + 128 * ch + 8 * n);
    v4u w2[4];
#pragma unroll
    for (int i = 0; i < 4; ++i) w2[i] = __builtin_nontemporal_load(p2 + i * (D / 8));
    __builtin_amdgcn_sched_barrier(0);

    unsigned sel[4][2];
#pragma unroll
    for (int r = 0; r < 4; ++r) {
        const bool on = (r == (jp >> 1));
        sel[r][0] = on ? ((jp & 1) ? 0x01000c0cu : 0x0c0c0100u) : 0x0c0c0c0cu;
        sel[r][1] = on ? ((jp & 1) ? 0x03020c0cu : 0x0c0c0302u) : 0x0c0c0c0cu;
    }
    f32x4 acc0 = {0.f, 0.f, 0.f, 0.f}, acc1 = {0.f, 0.f, 0.f, 0.f};
#pragma unroll
    for (int i = 0; i < 8; ++i) {
        v4u a0, a1;
#pragma unroll
        for (int r = 0; r < 4; ++r) {
            a0[r] = __builtin_amdgcn_perm(xa[i >> 1], xa[i >> 1], sel[r][i & 1]);
            a1[r] = __builtin_amdgcn_perm(xb[i >> 1], xb[i >> 1], sel[r][i & 1]);
        }
        acc0 = mfma(a0, w1[i], acc0);
        acc1 = mfma(a1, w1[i], acc1);
    }
    red[(w * 2 + 0) * 64 + lane] = acc0;
    red[(w * 2 + 1) * 64 + lane] = acc1;
    STAMP(2);
    __syncthreads();
    const unsigned tag = epoch + 1u;
    const float* rf = (const float*)red;
    if (tid < 256) {
        float gs = 0.f, us = 0.f;
#pragma unroll
        for (int ww = 0; ww < 8; ++ww) {
            const int idx = ww * 512 + (tid >> 7) * 256 + ((tid >> 5) & 3) * 64 + (tid & 31);
            gs += rf[idx];
            us += rf[idx + 32];
        }
        v4u o = {as_u(gs), tag, as_u(us), tag};
        __builtin_amdgcn_raw_buffer_store_b128(o, rs, S1_OFF + (bid * 256 + tid) * 16, 0, AUX_SC1);
        STAMP(3);
        const int o1 = S1_OFF + (((bid & ~3) | ((q + 1) & 3)) * 256 + tid) * 16;
        const int o2 = S1_OFF + (((bid & ~3) | ((q + 2) & 3)) * 256 + tid) * 16;
        const int o3 = S1_OFF + (((bid & ~3) | ((q + 3) & 3)) * 256 + tid) * 16;
        v4u a, b, c;
        for (unsigned spin = 0; spin < SPIN_CAP; ++spin) {
            asm volatile("" ::: "memory");
            a = __builtin_amdgcn_raw_buffer_load_b128(rs, o1, 0, AUX_SC1);
            b = __builtin_amdgcn_raw_buffer_load_b128(rs, o2, 0, AUX_SC1);
            c = __builtin_amdgcn_raw_buffer_load_b128(rs, o3, 0, AUX_SC1);
            const bool ok = a[1] == tag && a[3] == tag && b[1] == tag && b[3] == tag && c[1] == tag && c[3] == tag;
            if (__all(ok)) break;
        }
        STAMP(4);
        float gq[4], uq[4];
        gq[0] = gs; uq[0] = us; gq[1] = as_f(a[0]); uq[1] = as_f(a[2]); gq[2] = as_f(b[0]); uq[2] = as_f(b[2]);
        gq[3] = as_f(c[0]); uq[3] = as_f(c[2]);
        const float gt = (gq[0] + gq[2]) + (gq[1] + gq[3]);
        const float ut = (uq[0] + uq[2]) + (uq[1] + uq[3]);
        const float h = ut / (1.0f + __expf(-gt));
        const int hb = 2 * (tid >> 7) + ((tid >> 6) & 1), hf = 8 * ((tid >> 2) & 7) + 4 * ((tid >> 5) & 1) + (tid & 3);
        hs[hb * 64 + hf] = (u16)bf16_rne(h);
    }
    __syncthreads();
    {
        const v2u ha = *(const v2u*)&hs[b2 * 64 + 16 * rg + 4 * q4];
        const v2u hb = *(const v2u*)&hs[(b2 + 2) * 64 + 16 * rg + 4 * q4];
        acc0 = f32x4{0.f, 0.f, 0.f, 0.f};
        acc1 = f32x4{0.f, 0.f, 0.f, 0.f};
#pragma unroll
        for (int i = 0; i < 4; ++i) {
            v4u a0, a1;
#pragma unroll
            for (int r = 0; r < 4; ++r) {
                a0[r] = __builtin_amdgcn_perm(ha[i >> 1], ha[i >> 1], sel[r][i & 1]);
                a1[r] = __builtin_amdgcn_perm(hb[i >> 1], hb[i >> 1], sel[r][i & 1]);
            }
            acc0 = mfma(a0, w2[i], acc0);
            acc1 = mfma(a1, w2[i], acc1);
        }
        red[(w * 2 + 0) * 64 + lane] = acc0;
        red[(w * 2 + 1) * 64 + lane] = acc1;
    }
    STAMP(5);
    __syncthreads();
    {
        const int b = tid >> 7, col = 2 * (tid & 127);
        const int cch = col >> 7, cn = (col >> 3) & 15, hi1 = (col >> 2) & 1, rp = (col >> 1) & 1;
        const int l2 = ((b & 1) * 2 + hi1) * 16 + cn;
        f32x2 v = {0.f, 0.f};
#pragma unroll
        for (int r = 0; r < 4; ++r) v += *(const f32x2*)&rf[(((r * 2 + cch) * 2 + (b >> 1)) * 64 + l2) * 4 + 2 * rp];
        v4u o = {as_u(v[0]), tag, as_u(v[1]), tag};
        __builtin_amdgcn_raw_buffer_store_b128(o, rs, S2_OFF + ((q * NG + g) * 512 + tid) * 16, 0, AUX_SC1);
    }
    STAMP(6);
    if (g < 32) {
        const int pp = tid & 15, gsl = tid >> 4;
        const int o1 = S2_OFF + ((q * NG + gsl) * 512 + 16 * g + pp) * 16;
        const bool two = w < 6;
        const int o2 = two ? o1 + 32 * 512 * 16 : o1;
        v4u a, b;
        for (unsigned spin = 0; spin < SPIN_CAP; ++spin) {
            asm volatile("" ::: "memory");
            a = __builtin_amdgcn_raw_buffer_load_b128(rs, o1, 0, AUX_SC1);
            b = __builtin_amdgcn_raw_buffer_load_b128(rs, o2, 0, AUX_SC1);
            const bool ok = a[1] == tag && a[3] == tag && b[1] == tag && b[3] == tag;
            if (__all(ok)) break;
        }
        STAMP(7);
        float v0 = as_f(a[0]), v1 = as_f(a[2]);
        if (two) { v0 += as_f(b[0]); v1 += as_f(b[2]); }
        v0 += __shfl_xor(v0, 16); v1 += __shfl_xor(v1, 16);
        v0 += __shfl_xor(v0, 32); v1 += __shfl_xor(v1, 32);
        if (lane < 16) { fin[w * 32 + 2 * pp] = v0; fin[w * 32 + 2 * pp + 1] = v1; }
        __syncthreads();
        if (tid < 16) {
            float s0 = 0.f, s1 = 0.f;
#pragma unroll
            for (int ww = 0; ww < 8; ++ww) { s0 += fin[ww * 32 + 2 * tid]; s1 += fin[ww * 32 + 2 * tid + 1]; }
            const int p = 16 * g + tid, b = p >> 7, col = 2 * (p & 127);
            *(unsigned*)(out + b * D + 256 * q + col) = bf16_rne(s0) | (bf16_rne(s1) << 16);
            if (bid == 0 && tid == 0) __builtin_amdgcn_raw_buffer_store_b32(tag, rs, 0, 0, AUX_SC1);
        }
    }
    STAMP(8);
}
}

extern "C" void kernel_launch(void* const* d_in, const int* in_sizes, int n_in, void* d_out, int out_size, void* d_ws,
                              size_t ws_size, hipStream_t stream) {
    using namespace glu;
    glu_kernel<<<NWG, NT, 0, stream>>>((const u16*)d_in[0], (const u16*)d_in[1], (const u16*)d_in[2],
                                       (const u16*)d_in[3], (u16*)d_out, (unsigned char*)d_ws);
}
```

```cpp
#include <hip/hip_runtime.h>
#include <stdint.h>

#define WD_POS 1
#define WD_DELAY 16
#define CODE_PF 1
#define STAGGER 4

namespace glu {
constexpr int B = 4, D = 1024, F = 3584;
constexpr int NT2 = F / 128;
constexpr int NWG = NT2 * 8;
constexpr int NG = F / 64;
constexpr int NT = 512;
constexpr int S1_OFF = 4096;
constexpr int S2_OFF = S1_OFF + NWG * 128 * 16;
constexpr int WS_BYTES = S2_OFF + NWG * 256 * 16;
constexpr int OOB = 0x7FFFFF00;
constexpr int STAMP_OFF = 64 << 20;
constexpr int AUX_SC1 = 16;
constexpr unsigned SPIN_CAP = 1u << 17;
constexpr int CODE_LINES = 24;
constexpr int WOUT = WD_POS == 3 ? 4 : 0;

typedef unsigned v4u __attribute__((ext_vector_type(4)));
typedef unsigned v2u __attribute__((ext_vector_type(2)));
typedef float f32x4 __attribute__((ext_vector_type(4)));
typedef float f32x2 __attribute__((ext_vector_type(2)));
typedef short bf16x4 __attribute__((ext_vector_type(4)));
typedef __bf16 bf2 __attribute__((ext_vector_type(2)));
typedef unsigned short u16;

__device__ __forceinline__ f32x4 mfma16(v2u a, unsigned b0, unsigned b1, f32x4 c) {
    return __builtin_amdgcn_mfma_f32_16x16x16bf16_1k(__builtin_bit_cast(bf16x4, a), __builtin_bit_cast(bf16x4, v2u{b0, b1}), c, 0, 0, 0);
}
__device__ __forceinline__ unsigned cvt_pk(float lo, float hi) {
    return __builtin_bit_cast(unsigned, bf2{(__bf16)lo, (__bf16)hi});
}
__device__ __forceinline__ float as_f(unsigned u) { return __builtin_bit_cast(float, u); }
__device__ __forceinline__ unsigned as_u(float f) { return __builtin_bit_cast(unsigned, f); }
template <int CTRL> __device__ __forceinline__ float add_dpp(float x) {
    return x + as_f(__builtin_amdgcn_update_dpp(0, as_u(x), CTRL, 0xf, 0xf, false));
}
struct Tag { unsigned b0, b1, b2, b3, all, t; };
__device__ __forceinline__ v4u pack_unit(f32x4 v, const Tag& t) {
    return v4u{(as_u(v[0]) & 0xFFFFFF00u) | t.b0, (as_u(v[1]) & 0xFFFFFF00u) | t.b1, (as_u(v[2]) & 0xFFFFFF00u) | t.b2,
               (as_u(v[3]) & 0xFFFFFF00u) | t.b3};
}
#define STR2(x) #x
#define STR(x) STR2(x)
#define POLL_STEP(R0, R1, R2, R3, RT)                                \
    "s_waitcnt vmcnt(1)\n\t"                                         \
    "v_perm_b32 %[c], " R1 ", " R0 ", %[sel0]\n\t"                   \
    "v_perm_b32 %[d], " R3 ", " R2 ", %[sel1]\n\t"                   \
    "v_or_b32 %[c], %[c], %[d]\n\t"                                  \
    "v_cmp_eq_u32 vcc, %[tag], %[c]\n\t"                             \
    "s_andn2_b64 %[nw], vcc, %[done]\n\t"                            \
    "s_or_b64 %[done], %[done], vcc\n\t"                             \
    "v_cndmask_b32 %[g0], %[g0], " R0 ", %[nw]\n\t"                  \
    "v_cndmask_b32 %[g1], %[g1], " R1 ", %[nw]\n\t"                  \
    "v_cndmask_b32 %[g2], %[g2], " R2 ", %[nw]\n\t"                  \
    "v_cndmask_b32 %[g3], %[g3], " R3 ", %[nw]\n\t"                  \
    "s_cmp_eq_u64 %[done], -1\n\t"                                   \
    "s_cbranch_scc1 2f\n\t"                                          \
    "v_cndmask_b32 %[o], %[off], %[oob], %[done]\n\t"                \
    "buffer_load_dwordx4 " RT ", %[o], %[rs], 0 offen sc1\n\t"
__device__ __forceinline__ v4u gather_unit(__amdgpu_buffer_rsrc_t rs, int off, unsigned tag_all) {
    unsigned g0 = 0, g1 = 0, g2 = 0, g3 = 0, o, c, d, spin;
    unsigned long long done = 0, nw;
    asm volatile(
        "buffer_load_dwordx4 v[248:251], %[off], %[rs], 0 offen sc1\n\t"
        "s_sleep " STR(STAGGER) "\n\t"
        "buffer_load_dwordx4 v[252:255], %[off], %[rs], 0 offen sc1\n\t"
        "s_mov_b32 %[spin], %[cap]\n"
        "1:\n\t"
        POLL_STEP("v248", "v249", "v250", "v251", "v[248:251]")
        POLL_STEP("v252", "v253", "v254", "v255", "v[252:255]")
        "s_sub_u32 %[spin], %[spin], 1\n\t"
        "s_cbranch_scc0 1b\n"
        "2:"
        : [g0] "+v"(g0), [g1] "+v"(g1), [g2] "+v"(g2), [g3] "+v"(g3), [o] "=&v"(o), [c] "=&v"(c), [d] "=&v"(d),
          [spin] "=&s"(spin), [done] "+s"(done), [nw] "=&s"(nw)
        : [off] "v"(off), [oob] "v"(OOB), [rs] "s"(rs), [tag] "s"(tag_all), [sel0] "s"(0x0c0c0400u), [sel1] "s"(0x04000c0cu),
          [cap] "i"(SPIN_CAP)
        : "vcc", "scc", "memory", "v248", "v249", "v250", "v251", "v252", "v253", "v254", "v255");
    return v4u{g0, g1, g2, g3};
}

#define STAMP(i) do { } while (0)
#define STAMPE(i) do { } while (0)

__global__ void __launch_bounds__(NT, 2)
glu_kernel(const u16* __restrict__ x, const u16* __restrict__ wg, const u16* __restrict__ wu,
           const u16* __restrict__ wd, u16* __restrict__ out, unsigned char* __restrict__ ws) {
    __shared__ f32x4 red[8 * 2 * 68];
    __shared__ f32x4 red2[8 * 4 * 68];
    __shared__ float fin[8 * 32];
    const int tid = threadIdx.x, lane = tid & 63;
    STAMPE(0);
    unsigned pf = 0;
    asm volatile("v_cmp_gt_u32 vcc, %2, %3\n\t"
                 "s_and_saveexec_b64 s[42:43], vcc\n\t"
                 "s_getpc_b64 s[40:41]\n\t"
                 "global_load_dword %0, %1, s[40:41]\n\t"
                 "s_mov_b64 exec, s[42:43]"
                 : "=v"(pf) : "v"(tid * 128), "i"(CODE_LINES), "v"(tid) : "s40", "s41", "s42", "s43", "vcc");
    __builtin_amdgcn_sched_barrier(0);
    const int bid = blockIdx.x, T = bid >> 3, j = bid & 7;
    const int M = j >> 2, q = j & 3;
    const int hh = j & 1, cq = j >> 1;
    const int g = 2 * T + hh;
    const int q4 = lane >> 4, n = lane & 15, ab = n >> 2, jp = lane & 3;

    const __amdgpu_buffer_rsrc_t rs = __builtin_amdgcn_make_buffer_rsrc((void*)ws, 0, WS_BYTES, 0x00020000);
    const __amdgpu_buffer_rsrc_t rw = __builtin_amdgcn_make_buffer_rsrc((void*)(M ? wu : wg), 0, D * F * 2, 0x00020000);
    const __amdgpu_buffer_rsrc_t rd = __builtin_amdgcn_make_buffer_rsrc((void*)wd, 0, F * D * 2, 0x00020000);
    const __amdgpu_buffer_rsrc_t rx = __builtin_amdgcn_make_buffer_rsrc((void*)x, 0, B * D * 2, 0x00020000);

    const int voff1 = (tid >> 4) * (8 * F * 2) + n * 16;
    const int soff1 = q * (256 * F * 2) + T * 256;
    const int xoff = ab * (D * 2) + (tid >> 4) * 16;
    v4u w1[8], w2[4], xa;
    unsigned epoch, st1;
    asm volatile("buffer_load_dword %1, off, %3, 0 sc1\n\t"
                 "buffer_load_dwordx4 %0, %2, %4, %5 offen"
                 : "=&v"(xa), "=&v"(epoch) : "v"(xoff), "s"(rs), "s"(rx), "s"(q * 512) : "memory");
    asm volatile("buffer_load_dwordx4 %[w0], %[vo], %[rw], %[so] offen nt\n\t"
                 "s_add_u32 %[t], %[so], 0x1c00\n\t"
                 "buffer_load_dwordx4 %[w1], %[vo], %[rw], %[t] offen nt\n\t"
                 "s_add_u32 %[t], %[t], 0x1c00\n\t"
                 "buffer_load_dwordx4 %[w2], %[vo], %[rw], %[t] offen nt\n\t"
                 "s_add_u32 %[t], %[t], 0x1c00\n\t"
                 "buffer_load_dwordx4 %[w3], %[vo], %[rw], %[t] offen nt\n\t"
                 "s_add_u32 %[t], %[t], 0x1c00\n\t"
                 "buffer_load_dwordx4 %[w4], %[vo], %[rw], %[t] offen nt\n\t"
                 "s_add_u32 %[t], %[t], 0x1c00\n\t"
                 "buffer_load_dwordx4 %[w5], %[vo], %[rw], %[t] offen nt\n\t"
                 "s_add_u32 %[t], %[t], 0x1c00\n\t"
                 "buffer_load_dwordx4 %[w6], %[vo], %[rw], %[t] offen nt\n\t"
                 "s_add_u32 %[t], %[t], 0x1c00\n\t"
                 "buffer_load_dwordx4 %[w7], %[vo], %[rw], %[t] offen nt"
                 : [w0] "=&v"(w1[0]), [w1] "=&v"(w1[1]), [w2] "=&v"(w1[2]), [w3] "=&v"(w1[3]), [w4] "=&v"(w1[4]), [w5] "=&v"(w1[5]),
                   [w6] "=&v"(w1[6]), [w7] "=&v"(w1[7]), [t] "=&s"(st1)
                 : [vo] "v"(voff1), [rw] "s"(rw), [so] "s"(soff1) : "scc", "memory");
    __builtin_amdgcn_sched_barrier(0);
    const int w = __builtin_amdgcn_readfirstlane(tid >> 6);
    const int voff2 = (8 * w + 2 * q4) * (D * 2) + n * 16;
    const int soff2 = g * (64 * D * 2) + cq * 512;
#define LOAD_WD() asm volatile("buffer_load_dwordx4 %0, %4, %5, %6 offen nt\n\t" \
                               "buffer_load_dwordx4 %1, %4, %5, %6 offen offset:256 nt\n\t" \
                               "buffer_load_dwordx4 %2, %4, %5, %6 offen offset:2048 nt\n\t" \
                               "buffer_load_dwordx4 %3, %4, %5, %6 offen offset:2304 nt" \
                               : "=&v"(w2[0]), "=&v"(w2[1]), "=&v"(w2[2]), "=&v"(w2[3]) \
                               : "v"(voff2), "s"(rd), "s"(soff2) : "memory")
    const unsigned on0 = (jp & 1) ? 0x01000c0cu : 0x0c0c0100u, on1 = (jp & 1) ? 0x03020c0cu : 0x0c0c0302u;
    unsigned sel[2][2];
    sel[0][0] = (jp >> 1) ? 0x0c0c0c0cu : on0; sel[0][1] = (jp >> 1) ? 0x0c0c0c0cu : on1;
    sel[1][0] = (jp >> 1) ? on0 : 0x0c0c0c0cu; sel[1][1] = (jp >> 1) ? on1 : 0x0c0c0c0cu;
    int pub1 = S1_OFF + (bid * 128 + (tid & 127)) * 16;
    int get1 = S1_OFF + ((T * 8 + (q4 & 1) * 4 + jp) * 128 + (hh * 8 + w) * 8 + (q4 >> 1) * 4 + ab) * 16;
    int pub2 = S2_OFF + ((cq * NG + g) * 256 + (tid & 255)) * 16;
    int get2 = S2_OFF + ((cq * NG + 8 * w + (lane >> 3)) * 256 + 8 * g + (lane & 7)) * 16;
    const int wr = (w * 2) * 68 + q4 * 17 + n, wr2 = (w * 4) * 68 + q4 * 17 + n;
    int rd1 = ((tid >> 2) & 1) * 68 + (tid & 3) * 17 + ((tid >> 3) & 15);
    int rd2 = (((tid >> 5) & 1) * 2 + (tid & 1)) * 68 + ((tid >> 6) & 3) * 17 + ((tid >> 1) & 15);
    asm volatile("" : "+v"(pub1), "+v"(get1), "+v"(pub2), "+v"(get2), "+v"(rd1), "+v"(rd2));
    asm volatile("s_waitcnt vmcnt(%2)" : "+v"(xa), "+v"(epoch) : "i"(8 + WOUT), "v"(pf));
    Tag tg;
    {
        const unsigned t = __builtin_amdgcn_readfirstlane(epoch) + 1u;
        tg.t = t;
        tg.b0 = t & 0xFFu; tg.b1 = ((t >> 8) + t) & 0xFFu; tg.b2 = ((t >> 16) + t) & 0xFFu; tg.b3 = ((t >> 24) + t) & 0xFFu;
        tg.all = tg.b0 | (tg.b1 << 8) | (tg.b2 << 16) | (tg.b3 << 24);
    }
    v2u af[8];
#pragma unroll
    for (int i = 0; i < 8; ++i) {
        af[i] = v2u{__builtin_amdgcn_perm(xa[i >> 1], xa[i >> 1], sel[0][i & 1]), __builtin_amdgcn_perm(xa[i >> 1], xa[i >> 1], sel[1][i & 1])};
        asm volatile("" : "+v"(af[i]));
    }
    f32x4 acc0 = {0.f, 0.f, 0.f, 0.f}, acc1 = {0.f, 0.f, 0.f, 0.f};
#pragma unroll
    for (int i = 0; i < 8; ++i) {
        asm volatile("s_waitcnt vmcnt(%1)" : "+v"(w1[i]) : "i"(7 - i + WOUT));
        acc0 = mfma16(af[i], w1[i][0], w1[i][1], acc0);
        acc1 = mfma16(af[i], w1[i][2], w1[i][3], acc1);
    }
    __builtin_amdgcn_sched_barrier(0);
    LOAD_WD();
    __builtin_amdgcn_sched_barrier(0);
    red[wr] = acc0;
    red[wr + 68] = acc1;
    STAMP(2);
    __syncthreads();
    if (tid < 128) {
        f32x4 v = red[rd1];
#pragma unroll
        for (int ww = 1; ww < 8; ++ww) v += red[ww * 2 * 68 + rd1];
        __builtin_amdgcn_raw_buffer_store_b128(pack_unit(v, tg), rs, pub1, 0, AUX_SC1);
    }
    STAMP(3);
    {
        const v4u a = gather_unit(rs, get1, tg.all);
        asm volatile("" : "+v"(w2[0]), "+v"(w2[1]), "+v"(w2[2]), "+v"(w2[3]));
        STAMP(4);
        auto s02 = __builtin_amdgcn_permlane16_swap(a[0] & 0xFFFFFF00u, a[2] & 0xFFFFFF00u, false, false);
        auto s13 = __builtin_amdgcn_permlane16_swap(a[1] & 0xFFFFFF00u, a[3] & 0xFFFFFF00u, false, false);
        const float g0 = add_dpp<0x4E>(add_dpp<0xB1>(as_f(s02[0]))), g1 = add_dpp<0x4E>(add_dpp<0xB1>(as_f(s13[0])));
        const float u0 = add_dpp<0x4E>(add_dpp<0xB1>(as_f(s02[1]))), u1 = add_dpp<0x4E>(add_dpp<0xB1>(as_f(s13[1])));
        const float h0 = u0 * __builtin_amdgcn_rcpf(1.0f + __expf(-g0));
        const float h1 = u1 * __builtin_amdgcn_rcpf(1.0f + __expf(-g1));
        const unsigned hp = cvt_pk(h0, h1);
        const v2u a0 = {__builtin_amdgcn_perm(hp, hp, sel[0][0]), __builtin_amdgcn_perm(hp, hp, sel[1][0])};
        const v2u a1 = {__builtin_amdgcn_perm(hp, hp, sel[0][1]), __builtin_amdgcn_perm(hp, hp, sel[1][1])};
        const f32x4 z = {0.f, 0.f, 0.f, 0.f};
        f32x4 c0 = mfma16(a0, w2[0][0], w2[0][1], z), c1 = mfma16(a0, w2[0][2], w2[0][3], z);
        f32x4 c2 = mfma16(a0, w2[1][0], w2[1][1], z), c3 = mfma16(a0, w2[1][2], w2[1][3], z);
        c0 = mfma16(a1, w2[2][0], w2[2][1], c0); c1 = mfma16(a1, w2[2][2], w2[2][3], c1);
        c2 = mfma16(a1, w2[3][0], w2[3][1], c2); c3 = mfma16(a1, w2[3][2], w2[3][3], c3);
        red2[wr2] = c0; red2[wr2 + 68] = c1; red2[wr2 + 136] = c2; red2[wr2 + 204] = c3;
    }
    STAMP(5);
    __syncthreads();
    if (tid < 256) {
        f32x4 v = red2[rd2];
#pragma unroll
        for (int ww = 1; ww < 8; ++ww) v += red2[ww * 4 * 68 + rd2];
        __builtin_amdgcn_raw_buffer_store_b128(pack_unit(v, tg), rs, pub2, 0, AUX_SC1);
    }
    STAMP(6);
    if (g < 32) {
        if (w < 7) {
            const v4u a = gather_unit(rs, get2, tg.all);
            STAMP(7);
            auto s02 = __builtin_amdgcn_permlane32_swap(a[0] & 0xFFFFFF00u, a[2] & 0xFFFFFF00u, false, false);
            auto s13 = __builtin_amdgcn_permlane32_swap(a[1] & 0xFFFFFF00u, a[3] & 0xFFFFFF00u, false, false);
            const float p = as_f(s02[0]) + as_f(s02[1]), r = as_f(s13[0]) + as_f(s13[1]);
            auto sx = __builtin_amdgcn_permlane16_swap(as_u(p), as_u(r), false, false);
            const float v = add_dpp<0x128>(as_f(sx[0]) + as_f(sx[1]));
            if ((lane & 8) == 0) fin[w * 32 + (lane & 7) * 4 + (lane >> 4)] = v;
        }
        __syncthreads();
        if (tid < 32) {
            float s = fin[tid];
#pragma unroll
            for (int ww = 1; ww < 7; ++ww) s += fin[ww * 32 + tid];
            const int e = 32 * g + tid, b = e >> 8, col = e & 255;
            ((__bf16*)out)[b * D + 256 * cq + col] = (__bf16)s;
            if (bid == 0 && tid == 0) __builtin_amdgcn_raw_buffer_store_b32(tg.t, rs, 0, 0, AUX_SC1);
        }
    }
    STAMPE(8);
}
}

extern "C" void kernel_launch(void* const* d_in, const int* in_sizes, int n_in, void* d_out, int out_size, void* d_ws,
                              size_t ws_size, hipStream_t stream) {
    using namespace glu;
    glu_kernel<<<NWG, NT, 0, stream>>>((const u16*)d_in[0], (const u16*)d_in[1], (const u16*)d_in[2],
                                       (const u16*)d_in[3], (u16*)d_out, (unsigned char*)d_ws);
}
```

```cpp
#include <hip/hip_runtime.h>
#include <stdint.h>

namespace glu {
constexpr int B = 4, D = 1024, F = 3584;
constexpr int NWG = (F / 128) * 8;
constexpr int NG = F / 64;
constexpr int NT = 512;
constexpr int S1_OFF = 4096;
constexpr int S2_OFF = S1_OFF + NWG * 128 * 16;
constexpr int WS_BYTES = S2_OFF + NWG * 256 * 16;
constexpr int OOB = 0x7FFFFF00;
constexpr int AUX_SC1 = 16;
constexpr unsigned SPIN_CAP = 1u << 17;
constexpr unsigned POLL1_TICKS = 375, POLL1_SLOW_TICKS = 405, POLL2_TICKS = 540;

typedef unsigned v4u __attribute__((ext_vector_type(4)));
typedef unsigned v2u __attribute__((ext_vector_type(2)));
typedef float f32x4 __attribute__((ext_vector_type(4)));
typedef short bf16x4 __attribute__((ext_vector_type(4)));
typedef __bf16 bf2 __attribute__((ext_vector_type(2)));
typedef unsigned short u16;

__device__ __forceinline__ f32x4 mfma16(v2u a, unsigned b0, unsigned b1, f32x4 c) {
    return __builtin_amdgcn_mfma_f32_16x16x16bf16_1k(__builtin_bit_cast(bf16x4, a), __builtin_bit_cast(bf16x4, v2u{b0, b1}), c, 0, 0, 0);
}
__device__ __forceinline__ unsigned cvt_pk(float lo, float hi) { return __builtin_bit_cast(unsigned, bf2{(__bf16)lo, (__bf16)hi}); }
__device__ __forceinline__ float as_f(unsigned u) { return __builtin_bit_cast(float, u); }
__device__ __forceinline__ unsigned as_u(float f) { return __builtin_bit_cast(unsigned, f); }
template <int CTRL> __device__ __forceinline__ float add_dpp(float x) {
    return x + as_f(__builtin_amdgcn_update_dpp(0, as_u(x), CTRL, 0xf, 0xf, false));
}
__device__ __forceinline__ void wait_ticks(unsigned t0, unsigned ticks) {
    while ((unsigned)__builtin_amdgcn_s_memrealtime() - t0 < ticks) __builtin_amdgcn_s_sleep(1);
}

struct Tag { unsigned b0, b1, b2, b3, all, t; };
__device__ __forceinline__ v4u pack_unit(f32x4 v, const Tag& t) {
    return v4u{(as_u(v[0]) & 0xFFFFFF00u) | t.b0, (as_u(v[1]) & 0xFFFFFF00u) | t.b1, (as_u(v[2]) & 0xFFFFFF00u) | t.b2,
               (as_u(v[3]) & 0xFFFFFF00u) | t.b3};
}

#define POLL_STEP(R0, R1, R2, R3, RT)                                \
    "s_waitcnt vmcnt(1)\n\t"                                         \
    "v_perm_b32 %[c], " R1 ", " R0 ", %[sel0]\n\t"                   \
    "v_perm_b32 %[d], " R3 ", " R2 ", %[sel1]\n\t"                   \
    "v_or_b32 %[c], %[c], %[d]\n\t"                                  \
    "v_cmp_eq_u32 vcc, %[tag], %[c]\n\t"                             \
    "s_andn2_b64 %[nw], vcc, %[done]\n\t"                            \
    "s_or_b64 %[done], %[done], vcc\n\t"                             \
    "v_cndmask_b32 %[g0], %[g0], " R0 ", %[nw]\n\t"                  \
    "v_cndmask_b32 %[g1], %[g1], " R1 ", %[nw]\n\t"                  \
    "v_cndmask_b32 %[g2], %[g2], " R2 ", %[nw]\n\t"                  \
    "v_cndmask_b32 %[g3], %[g3], " R3 ", %[nw]\n\t"                  \
    "s_cmp_eq_u64 %[done], -1\n\t"                                   \
    "s_cbranch_scc1 2f\n\t"                                          \
    "v_cndmask_b32 %[o], %[off], %[oob], %[done]\n\t"                \
    "buffer_load_dwordx4 " RT ", %[o], %[rs], 0 offen sc1\n\t"
__device__ __forceinline__ v4u gather_unit(__amdgpu_buffer_rsrc_t rs, int off, unsigned tag_all) {
    unsigned g0, g1, g2, g3, o, c, d, spin;
    unsigned long long done, nw;
    asm volatile(
        "buffer_load_dwordx4 v[248:251], %[off], %[rs], 0 offen sc1\n\t"
        "s_waitcnt vmcnt(0)\n\t"
        "v_perm_b32 %[c], v249, v248, %[sel0]\n\t"
        "v_perm_b32 %[d], v251, v250, %[sel1]\n\t"
        "v_or_b32 %[c], %[c], %[d]\n\t"
        "v_cmp_eq_u32 vcc, %[tag], %[c]\n\t"
        "s_mov_b64 %[done], vcc\n\t"
        "v_mov_b32 %[g0], v248\n\t"
        "v_mov_b32 %[g1], v249\n\t"
        "v_mov_b32 %[g2], v250\n\t"
        "v_mov_b32 %[g3], v251\n\t"
        "s_cmp_eq_u64 %[done], -1\n\t"
        "s_cbranch_scc1 2f\n\t"
        "v_cndmask_b32 %[o], %[off], %[oob], %[done]\n\t"
        "buffer_load_dwordx4 v[248:251], %[o], %[rs], 0 offen sc1\n\t"
        "s_sleep 4\n\t"
        "buffer_load_dwordx4 v[252:255], %[o], %[rs], 0 offen sc1\n\t"
        "s_mov_b32 %[spin], %[cap]\n"
        "1:\n\t"
        POLL_STEP("v248", "v249", "v250", "v251", "v[248:251]")
        POLL_STEP("v252", "v253", "v254", "v255", "v[252:255]")
        "s_sub_u32 %[spin], %[spin], 1\n\t"
        "s_cbranch_scc0 1b\n"
        "2:"
        : [g0] "=&v"(g0), [g1] "=&v"(g1), [g2] "=&v"(g2), [g3] "=&v"(g3), [o] "=&v"(o), [c] "=&v"(c), [d] "=&v"(d),
          [spin] "=&s"(spin), [done] "=&s"(done), [nw] "=&s"(nw)
        : [off] "v"(off), [oob] "v"(OOB), [rs] "s"(rs), [tag] "s"(tag_all), [sel0] "s"(0x0c0c0400u), [sel1] "s"(0x04000c0cu),
          [cap] "i"(SPIN_CAP)
        : "vcc", "scc", "memory", "v248", "v249", "v250", "v251", "v252", "v253", "v254", "v255");
    return v4u{g0, g1, g2, g3};
}

__global__ void __launch_bounds__(NT, 2)
glu_kernel(const u16* __restrict__ x, const u16* __restrict__ wg, const u16* __restrict__ wu,
           const u16* __restrict__ wd, u16* __restrict__ out, unsigned char* __restrict__ ws) {
    __shared__ f32x4 red1[8 * 2 * 68];
    __shared__ f32x4 red2[8 * 4 * 68];
    __shared__ float fin[7 * 32];
    const int tid = threadIdx.x, lane = tid & 63;
    const int bid = blockIdx.x, T = bid >> 3, j = bid & 7;
    const int M = j >> 2, q = j & 3;
    const int hh = j & 1, cq = j >> 1;
    const int g = 2 * T + hh;
    const int q4 = lane >> 4, n = lane & 15, ab = n >> 2, jp = lane & 3;

    const __amdgpu_buffer_rsrc_t rs = __builtin_amdgcn_make_buffer_rsrc((void*)ws, 0, WS_BYTES, 0x00020000);
    const __amdgpu_buffer_rsrc_t rw = __builtin_amdgcn_make_buffer_rsrc((void*)(M ? wu : wg), 0, D * F * 2, 0x00020000);
    const __amdgpu_buffer_rsrc_t rd = __builtin_amdgcn_make_buffer_rsrc((void*)wd, 0, F * D * 2, 0x00020000);
    const __amdgpu_buffer_rsrc_t rx = __builtin_amdgcn_make_buffer_rsrc((void*)x, 0, B * D * 2, 0x00020000);
    const __amdgpu_buffer_rsrc_t ro = __builtin_amdgcn_make_buffer_rsrc((void*)out, 0, B * D * 2, 0x00020000);

    unsigned long long T0;
    asm volatile("s_memrealtime %0" : "=s"(T0) : "s"(ws), "s"(x) : "memory");

    const int voff1 = (tid >> 4) * (8 * F * 2) + n * 16;
    const int soff1 = q * (256 * F * 2) + T * 256;
    const int xoff = ab * (D * 2) + (tid >> 4) * 16;
    v4u w1[8], xa;
    unsigned epoch, st1;
    asm volatile("buffer_load_dword %1, off, %3, 0 sc1\n\t"
                 "buffer_load_dwordx4 %0, %2, %4, %5 offen"
                 : "=&v"(xa), "=&v"(epoch) : "v"(xoff), "s"(rs), "s"(rx), "s"(q * 512) : "memory");
    asm volatile("buffer_load_dwordx4 %[w0], %[vo], %[rw], %[so] offen nt\n\t"
                 "s_add_u32 %[t], %[so], 0x1c00\n\t"
                 "buffer_load_dwordx4 %[w1], %[vo], %[rw], %[t] offen nt\n\t"
                 "s_add_u32 %[t], %[t], 0x1c00\n\t"
                 "buffer_load_dwordx4 %[w2], %[vo], %[rw], %[t] offen nt\n\t"
                 "s_add_u32 %[t], %[t], 0x1c00\n\t"
                 "buffer_load_dwordx4 %[w3], %[vo], %[rw], %[t] offen nt\n\t"
                 "s_add_u32 %[t], %[t], 0x1c00\n\t"
                 "buffer_load_dwordx4 %[w4], %[vo], %[rw], %[t] offen nt\n\t"
                 "s_add_u32 %[t], %[t], 0x1c00\n\t"
                 "buffer_load_dwordx4 %[w5], %[vo], %[rw], %[t] offen nt\n\t"
                 "s_add_u32 %[t], %[t], 0x1c00\n\t"
                 "buffer_load_dwordx4 %[w6], %[vo], %[rw], %[t] offen nt\n\t"
                 "s_add_u32 %[t], %[t], 0x1c00\n\t"
                 "buffer_load_dwordx4 %[w7], %[vo], %[rw], %[t] offen nt"
                 : [w0] "=&v"(w1[0]), [w1] "=&v"(w1[1]), [w2] "=&v"(w1[2]), [w3] "=&v"(w1[3]), [w4] "=&v"(w1[4]), [w5] "=&v"(w1[5]),
                   [w6] "=&v"(w1[6]), [w7] "=&v"(w1[7]), [t] "=&s"(st1)
                 : [vo] "v"(voff1), [rw] "s"(rw), [so] "s"(soff1) : "scc", "memory");
    __builtin_amdgcn_sched_barrier(0);

    const int w = __builtin_amdgcn_readfirstlane(tid >> 6);
    const int voff2 = (8 * w + 2 * q4) * (D * 2) + n * 16;
    const int soff2 = g * (64 * D * 2) + cq * 512;
    const unsigned on0 = (jp & 1) ? 0x01000c0cu : 0x0c0c0100u, on1 = (jp & 1) ? 0x03020c0cu : 0x0c0c0302u;
    unsigned sel[2][2];
    sel[0][0] = (jp >> 1) ? 0x0c0c0c0cu : on0; sel[0][1] = (jp >> 1) ? 0x0c0c0c0cu : on1;
    sel[1][0] = (jp >> 1) ? on0 : 0x0c0c0c0cu; sel[1][1] = (jp >> 1) ? on1 : 0x0c0c0c0cu;
    int pub1 = S1_OFF + (bid * 128 + (tid & 127)) * 16;
    int get1 = S1_OFF + ((T * 8 + (q4 & 1) * 4 + jp) * 128 + (hh * 8 + w) * 8 + (q4 >> 1) * 4 + ab) * 16;
    int pub2 = S2_OFF + ((cq * NG + g) * 256 + (tid & 255)) * 16;
    int get2 = S2_OFF + ((cq * NG + 8 * w + (lane >> 3)) * 256 + 8 * g + (lane & 7)) * 16;
    int wr1 = (w * 2) * 68 + q4 * 17 + n, wr2 = (w * 4) * 68 + q4 * 17 + n;
    int rd1 = ((tid >> 2) & 1) * 68 + (tid & 3) * 17 + ((tid >> 3) & 15);
    int rd2 = (((tid >> 5) & 1) * 2 + (tid & 1)) * 68 + ((tid >> 6) & 3) * 17 + ((tid >> 1) & 15);
    asm volatile("" : "+v"(pub1), "+v"(get1), "+v"(pub2), "+v"(get2), "+v"(rd1), "+v"(rd2), "+v"(wr1), "+v"(wr2));

    asm volatile("s_waitcnt vmcnt(8) lgkmcnt(0)" : "+v"(xa), "+v"(epoch), "+s"(T0));
    const unsigned t0 = (unsigned)T0;
    Tag tg;
    {
        const unsigned t = __builtin_amdgcn_readfirstlane(epoch) + 1u;
        tg.t = t;
        tg.b0 = t & 0xFFu; tg.b1 = ((t >> 8) + t) & 0xFFu; tg.b2 = ((t >> 16) + t) & 0xFFu; tg.b3 = ((t >> 24) + t) & 0xFFu;
        tg.all = tg.b0 | (tg.b1 << 8) | (tg.b2 << 16) | (tg.b3 << 24);
        asm volatile("" : "+s"(tg.b0), "+s"(tg.b1), "+s"(tg.b2), "+s"(tg.b3), "+s"(tg.all), "+s"(tg.t));
    }
    v2u af[8];
#pragma unroll
    for (int i = 0; i < 8; ++i) {
        af[i] = v2u{__builtin_amdgcn_perm(xa[i >> 1], xa[i >> 1], sel[0][i & 1]), __builtin_amdgcn_perm(xa[i >> 1], xa[i >> 1], sel[1][i & 1])};
        asm volatile("" : "+v"(af[i]));
    }

    f32x4 acc0 = {0.f, 0.f, 0.f, 0.f}, acc1 = {0.f, 0.f, 0.f, 0.f};
#pragma unroll
    for (int i = 0; i < 8; ++i) {
        asm volatile("s_waitcnt vmcnt(%1)" : "+v"(w1[i]) : "i"(7 - i));
        acc0 = mfma16(af[i], w1[i][0], w1[i][1], acc0);
        acc1 = mfma16(af[i], w1[i][2], w1[i][3], acc1);
        __builtin_amdgcn_sched_barrier(0);
    }
#define WD_REGS "v232", "v233", "v234", "v235", "v236", "v237", "v238", "v239", "v240", "v241", "v242", "v243", "v244", "v245", "v246", "v247"
    asm volatile("buffer_load_dwordx4 v[232:235], %0, %1, %2 offen nt\n\t"
                 "buffer_load_dwordx4 v[236:239], %0, %1, %2 offen offset:256 nt\n\t"
                 "buffer_load_dwordx4 v[240:243], %0, %1, %2 offen offset:2048 nt\n\t"
                 "buffer_load_dwordx4 v[244:247], %0, %1, %2 offen offset:2304 nt"
                 :: "v"(voff2), "s"(rd), "s"(soff2) : "memory", WD_REGS);
    __builtin_amdgcn_sched_barrier(0);
    red1[wr1] = acc0;
    red1[wr1 + 68] = acc1;
    __syncthreads();
    if (tid < 128) {
        f32x4 v = red1[rd1];
#pragma unroll
        for (int ww = 1; ww < 8; ++ww) v += red1[ww * 2 * 68 + rd1];
        __builtin_amdgcn_raw_buffer_store_b128(pack_unit(v, tg), rs, pub1, 0, AUX_SC1);
    }

    wait_ticks(t0, (T & 3) == 1 ? POLL1_SLOW_TICKS : POLL1_TICKS);
    {
        const v4u a = gather_unit(rs, get1, tg.all);
        auto s02 = __builtin_amdgcn_permlane16_swap(a[0] & 0xFFFFFF00u, a[2] & 0xFFFFFF00u, false, false);
        auto s13 = __builtin_amdgcn_permlane16_swap(a[1] & 0xFFFFFF00u, a[3] & 0xFFFFFF00u, false, false);
        const float g0 = add_dpp<0x4E>(add_dpp<0xB1>(as_f(s02[0]))), g1 = add_dpp<0x4E>(add_dpp<0xB1>(as_f(s13[0])));
        const float u0 = add_dpp<0x4E>(add_dpp<0xB1>(as_f(s02[1]))), u1 = add_dpp<0x4E>(add_dpp<0xB1>(as_f(s13[1])));
        const float h0 = u0 * __builtin_amdgcn_rcpf(1.0f + __expf(-g0));
        const float h1 = u1 * __builtin_amdgcn_rcpf(1.0f + __expf(-g1));
        const unsigned hp = cvt_pk(h0, h1);
        const v2u a0 = {__builtin_amdgcn_perm(hp, hp, sel[0][0]), __builtin_amdgcn_perm(hp, hp, sel[1][0])};
        const v2u a1 = {__builtin_amdgcn_perm(hp, hp, sel[0][1]), __builtin_amdgcn_perm(hp, hp, sel[1][1])};
        f32x4 c0, c1, c2, c3;
        asm volatile("s_nop 1\n\t"
                     "v_mfma_f32_16x16x16_bf16 %0, %4, v[232:233], 0\n\t"
                     "v_mfma_f32_16x16x16_bf16 %1, %4, v[234:235], 0\n\t"
                     "v_mfma_f32_16x16x16_bf16 %2, %4, v[236:237], 0\n\t"
                     "v_mfma_f32_16x16x16_bf16 %3, %4, v[238:239], 0\n\t"
                     "v_mfma_f32_16x16x16_bf16 %0, %5, v[240:241], %0\n\t"
                     "v_mfma_f32_16x16x16_bf16 %1, %5, v[242:243], %1\n\t"
                     "v_mfma_f32_16x16x16_bf16 %2, %5, v[244:245], %2\n\t"
                     "v_mfma_f32_16x16x16_bf16 %3, %5, v[246:247], %3\n\t"
                     "s_nop 7"
                     : "=&v"(c0), "=&v"(c1), "=&v"(c2), "=&v"(c3) : "v"(a0), "v"(a1) : WD_REGS);
        red2[wr2] = c0; red2[wr2 + 68] = c1; red2[wr2 + 136] = c2; red2[wr2 + 204] = c3;
    }
    __syncthreads();
    if (tid < 256) {
        f32x4 v = red2[rd2];
#pragma unroll
        for (int ww = 1; ww < 8; ++ww) v += red2[ww * 4 * 68 + rd2];
        __builtin_amdgcn_raw_buffer_store_b128(pack_unit(v, tg), rs, pub2, 0, AUX_SC1);
    }

    if (g < 32) {
        if (w < 7) {
            wait_ticks(t0, POLL2_TICKS);
            const v4u a = gather_unit(rs, get2, tg.all);
            auto s02 = __builtin_amdgcn_permlane32_swap(a[0] & 0xFFFFFF00u, a[2] & 0xFFFFFF00u, false, false);
            auto s13 = __builtin_amdgcn_permlane32_swap(a[1] & 0xFFFFFF00u, a[3] & 0xFFFFFF00u, false, false);
            const float p = as_f(s02[0]) + as_f(s02[1]), r = as_f(s13[0]) + as_f(s13[1]);
            auto sx = __builtin_amdgcn_permlane16_swap(as_u(p), as_u(r), false, false);
            const float v = add_dpp<0x128>(as_f(sx[0]) + as_f(sx[1]));
            if ((lane & 8) == 0) fin[w * 32 + (lane & 7) * 4 + (lane >> 4)] = v;
        }
        __syncthreads();
        if (tid < 32) {
            float s = fin[tid];
#pragma unroll
            for (int ww = 1; ww < 7; ++ww) s += fin[ww * 32 + tid];
            const int e = 32 * g + tid, b = e >> 8, col = e & 255;
            __builtin_amdgcn_raw_buffer_store_b16(__builtin_bit_cast(u16, (__bf16)s), ro, (b * D + 256 * cq + col) * 2, 0, AUX_SC1);
            if (bid == 0 && tid == 0) __builtin_amdgcn_raw_buffer_store_b32(tg.t, rs, 0, 0, AUX_SC1);
        }
    }
}
}

extern "C" void kernel_launch(void* const* d_in, const int* in_sizes, int n_in, void* d_out, int out_size, void* d_ws,
                              size_t ws_size, hipStream_t stream) {
    using namespace glu;
    glu_kernel<<<NWG, NT, 0, stream>>>((const u16*)d_in[0], (const u16*)d_in[1], (const u16*)d_in[2],
                                       (const u16*)d_in[3], (u16*)d_out, (unsigned char*)d_ws);
}
```
